```python
import torch

def _softmax(x, axis=-1):
    return torch.softmax(x, dim=axis)
def _gelu(x):
    return torch.nn.functional.gelu(x, approximate="tanh")
def _silu(x):
    return torch.nn.functional.silu(x)
def _sigmoid(x):
    return torch.sigmoid(x)
def _layernorm(x, axis=-1, eps=1e-5):
    m = x.mean(dim=axis, keepdim=True)
    v = x.var(dim=axis, keepdim=True, unbiased=False)
    return (x - m) / torch.sqrt(v + eps)
def _scatter_add(tgt, idx, src, axis=0):
    return tgt.clone().index_add_(axis, idx, src)
def _segment_sum(src, seg, n):
    out = torch.zeros((n,) + src.shape[1:], dtype=src.dtype)
    return out.index_add_(0, seg, src)
def _pad(x, pw):
    flat = tuple(v for pair in reversed(pw) for v in pair)
    return torch.nn.functional.pad(x, flat)

kernel_name = 'standalone__layernorm_linear__sampling'

def setup_inputs():

    _w = (lambda a: (lambda t: t.to(torch.bfloat16) if t.is_floating_point() else t)(torch.as_tensor(a)))
    B, S, D = 1024, 8, 640
    return {
        "x": _w((lambda *s: torch.randn(*s, dtype=torch.bfloat16) * 0.05)(B, S, D)),
        "g": _w((lambda *s: torch.randn(*s, dtype=torch.bfloat16) * 0.05)(D) + 1.0),
        "b": _w((lambda *s: torch.randn(*s, dtype=torch.bfloat16) * 0.4)(D)),
        "w": _w((lambda *s: torch.randn(*s, dtype=torch.bfloat16) * 0.05)(D, D)),
    }

def reference(x, g, b, w):
    return (_layernorm(x, axis=-1) * g + b) @ w
```

```cpp
#include <hip/hip_runtime.h>
#include <hip/hip_bf16.h>
#include <stdint.h>

typedef __attribute__((ext_vector_type(8))) short bf16x8;
typedef __attribute__((ext_vector_type(4))) short s16x4;
typedef __attribute__((ext_vector_type(4))) float f32x4;
typedef __attribute__((ext_vector_type(2))) float f32x2;
typedef __attribute__((ext_vector_type(4))) unsigned u32x4;
typedef __attribute__((ext_vector_type(2))) unsigned u32x2;
typedef __attribute__((ext_vector_type(2))) __bf16 bf16x2_t;
typedef __attribute__((ext_vector_type(4))) short v4i16_t;
typedef unsigned short u16;

constexpr int KD = 640, ND = 640;
constexpr int BM = 64, BN = 320, BK = 64;
constexpr int A_BYTES = BM * KD * 2;
constexpr int W_BYTES = BK * BN * 2;

__device__ __forceinline__ float dpp_sum16(float v) {
  int x;
  x = __builtin_amdgcn_update_dpp(0, __builtin_bit_cast(int, v), 0xB1, 0xF, 0xF, true);
  v += __builtin_bit_cast(float, x);
  x = __builtin_amdgcn_update_dpp(0, __builtin_bit_cast(int, v), 0x4E, 0xF, 0xF, true);
  v += __builtin_bit_cast(float, x);
  x = __builtin_amdgcn_update_dpp(0, __builtin_bit_cast(int, v), 0x141, 0xF, 0xF, true);
  v += __builtin_bit_cast(float, x);
  x = __builtin_amdgcn_update_dpp(0, __builtin_bit_cast(int, v), 0x140, 0xF, 0xF, true);
  v += __builtin_bit_cast(float, x);
  return v;
}
__device__ __forceinline__ float bf_lo(unsigned u) { return __builtin_bit_cast(float, u << 16); }
__device__ __forceinline__ float bf_hi(unsigned u) { return __builtin_bit_cast(float, u & 0xffff0000u); }
__device__ __forceinline__ unsigned pack2(float a, float b) {
  f32x2 f = {a, b};
  bf16x2_t r = __builtin_convertvector(f, bf16x2_t);
  return __builtin_bit_cast(unsigned, r);
}

__global__ __launch_bounds__(512, 2) void ln_gemm_kernel(const u16* __restrict__ x, const u16* __restrict__ g,
                                                         const u16* __restrict__ b, const u16* __restrict__ w,
                                                         u16* __restrict__ out) {
  __shared__ __attribute__((aligned(16))) char smem[A_BYTES + 2 * W_BYTES];
  char* sA = smem;
  char* sW = smem + A_BYTES;
  const int tid = threadIdx.x, lane = tid & 63, wid = tid >> 6;
  const int bid = blockIdx.x, xcd = bid & 7, idx = bid >> 3;
  const int cb = idx & 1, rb = (idx >> 1) * 8 + xcd;
  const int row0 = rb * BM, col0 = cb * BN;

  const int wk = tid >> 3, wc = tid & 7;
  const u16* wsrc = w + (size_t)wk * ND + col0 + wc * 8;
  const int wkp = wk ^ (((wk >> 3) & 1) << 2) ^ (wc >> 1);
  const int wdst = (wc >> 1) * 2048 + wkp * 32 + (wc & 1) * 16;
  u32x4 wreg[5];
#pragma unroll
  for (int i = 0; i < 5; ++i) wreg[i] = *(const u32x4*)(wsrc + i * 64);

  const int cl = lane & 15, rq = lane >> 4;
  u32x4 xr[2][5], gr[5], br[5];
#pragma unroll
  for (int h = 0; h < 2; ++h)
#pragma unroll
    for (int i = 0; i < 5; ++i)
      xr[h][i] = *(const u32x4*)(x + (size_t)(row0 + wid * 8 + h * 4 + rq) * KD + (cl + 16 * i) * 8);
#pragma unroll
  for (int i = 0; i < 5; ++i) {
    gr[i] = *(const u32x4*)(g + (cl + 16 * i) * 8);
    br[i] = *(const u32x4*)(b + (cl + 16 * i) * 8);
  }
#pragma unroll
  for (int i = 0; i < 5; ++i) *(u32x4*)(sW + wdst + i * 8192) = wreg[i];
#pragma unroll
  for (int i = 0; i < 5; ++i) wreg[i] = *(const u32x4*)(wsrc + (size_t)BK * ND + i * 64);

#pragma unroll
  for (int h = 0; h < 2; ++h) {
    float s = 0.f;
#pragma unroll
    for (int i = 0; i < 5; ++i)
#pragma unroll
      for (int e = 0; e < 4; ++e) s += bf_lo(xr[h][i][e]) + bf_hi(xr[h][i][e]);
    s = dpp_sum16(s);
    const float mean = s * (1.0f / KD);
    float q = 0.f;
#pragma unroll
    for (int i = 0; i < 5; ++i)
#pragma unroll
      for (int e = 0; e < 4; ++e) {
        float d0 = bf_lo(xr[h][i][e]) - mean, d1 = bf_hi(xr[h][i][e]) - mean;
        q += d0 * d0; q += d1 * d1;
      }
    q = dpp_sum16(q);
    const float rstd = rsqrtf(q * (1.0f / KD) + 1e-5f);
    const int r = wid * 8 + h * 4 + rq;
#pragma unroll
    for (int i = 0; i < 5; ++i) {
      u32x4 o;
#pragma unroll
      for (int e = 0; e < 4; ++e) {
        float y0 = (bf_lo(xr[h][i][e]) - mean) * rstd * bf_lo(gr[i][e]) + bf_lo(br[i][e]);
        float y1 = (bf_hi(xr[h][i][e]) - mean) * rstd * bf_hi(gr[i][e]) + bf_hi(br[i][e]);
        o[e] = pack2(y0, y1);
      }
      *(u32x4*)(sA + r * 1280 + ((cl ^ (r & 15)) + 16 * i) * 16) = o;
    }
  }
  __syncthreads();

  const int mq = wid >> 2, nq = wid & 3;
  const int fg = lane >> 4, fq = (lane & 15) >> 2, fp = lane & 3;
  f32x4 acc[2][5];
#pragma unroll
  for (int m = 0; m < 2; ++m)
#pragma unroll
    for (int n = 0; n < 5; ++n) acc[m][n] = (f32x4){0.f, 0.f, 0.f, 0.f};
  const int ar = mq * 32 + (lane & 15);
  const char* aBase = sA + ar * 1280;

  for (int it = 0; it < KD / BK; ++it) {
    const char* wb = sW + (it & 1) * W_BYTES;
#pragma unroll
    for (int s = 0; s < 2; ++s) {
      const int c = (it * 2 + s) * 4 + fg;
      const int co = ((c & ~15) | ((c & 15) ^ (ar & 15))) * 16;
      bf16x8 af[2];
      af[0] = *(const bf16x8*)(aBase + co);
      af[1] = *(const bf16x8*)(aBase + 16 * 1280 + co);
#pragma unroll
      for (int nf = 0; nf < 5; ++nf) {
        const int nb = nq * 5 + nf;
        const int k0 = 32 * s + 8 * fg + fq;
        const int kp0 = k0 ^ ((fg & 1) << 2) ^ (nb & 3);
        const int kp1 = (k0 + 4) ^ ((fg & 1) << 2) ^ (nb & 3);
        s16x4 lo = __builtin_bit_cast(s16x4, __builtin_amdgcn_ds_read_tr16_b64_v4i16(
            (__attribute__((address_space(3))) v4i16_t*)(wb + nb * 2048 + kp0 * 32 + fp * 8)));
        s16x4 hi = __builtin_bit_cast(s16x4, __builtin_amdgcn_ds_read_tr16_b64_v4i16(
            (__attribute__((address_space(3))) v4i16_t*)(wb + nb * 2048 + kp1 * 32 + fp * 8)));
        bf16x8 bfr = {lo[0], lo[1], lo[2], lo[3], hi[0], hi[1], hi[2], hi[3]};
        acc[0][nf] = __builtin_amdgcn_mfma_f32_16x16x32_bf16(bfr, af[0], acc[0][nf], 0, 0, 0);
        acc[1][nf] = __builtin_amdgcn_mfma_f32_16x16x32_bf16(bfr, af[1], acc[1][nf], 0, 0, 0);
      }
    }
    if (it + 1 < KD / BK) {
      char* wn = sW + ((it + 1) & 1) * W_BYTES;
#pragma unroll
      for (int i = 0; i < 5; ++i) *(u32x4*)(wn + wdst + i * 8192) = wreg[i];
      if (it + 2 < KD / BK) {
#pragma unroll
        for (int i = 0; i < 5; ++i) wreg[i] = *(const u32x4*)(wsrc + (size_t)(it + 2) * BK * ND + i * 64);
      }
    }
    __syncthreads();
  }

#pragma unroll
  for (int m = 0; m < 2; ++m)
#pragma unroll
    for (int n = 0; n < 5; ++n) {
      u32x2 o;
      o[0] = pack2(acc[m][n][0], acc[m][n][1]);
      o[1] = pack2(acc[m][n][2], acc[m][n][3]);
      *(u32x2*)(out + (size_t)(row0 + mq * 32 + m * 16 + (lane & 15)) * ND + col0 + nq * 80 + n * 16 + fg * 4) = o;
    }
}

extern "C" void kernel_launch(void* const* d_in, const int* in_sizes, int n_in,
                              void* d_out, int out_size, void* d_ws, size_t ws_size,
                              hipStream_t stream) {
  const u16* x = (const u16*)d_in[0];
  const u16* g = (const u16*)d_in[1];
  const u16* b = (const u16*)d_in[2];
  const u16* w = (const u16*)d_in[3];
  u16* out = (u16*)d_out;
  const int M = in_sizes[0] / KD;
  ln_gemm_kernel<<<dim3((M / BM) * (ND / BN)), dim3(512), 0, stream>>>(x, g, b, w, out);
}
```

```cpp
#include <hip/hip_runtime.h>
#include <hip/hip_bf16.h>
#include <stdint.h>

typedef __attribute__((ext_vector_type(8))) short bf16x8;
typedef __attribute__((ext_vector_type(4))) short s16x4;
typedef __attribute__((ext_vector_type(4))) float f32x4;
typedef __attribute__((ext_vector_type(2))) float f32x2;
typedef __attribute__((ext_vector_type(4))) unsigned u32x4;
typedef __attribute__((ext_vector_type(2))) unsigned u32x2;
typedef __attribute__((ext_vector_type(2))) __bf16 bf16x2_t;
typedef __attribute__((ext_vector_type(4))) short v4i16_t;
typedef unsigned short u16;

constexpr int KD = 640, ND = 640;
constexpr int BM = 64, BN = 320, BK = 32, NST = 4, NIT = KD / BK;
constexpr int A_BYTES = BM * KD * 2;
constexpr int W_BYTES = BK * BN * 2;

__device__ __forceinline__ float dpp_sum16(float v) {
  int x;
  x = __builtin_amdgcn_update_dpp(0, __builtin_bit_cast(int, v), 0xB1, 0xF, 0xF, true);
  v += __builtin_bit_cast(float, x);
  x = __builtin_amdgcn_update_dpp(0, __builtin_bit_cast(int, v), 0x4E, 0xF, 0xF, true);
  v += __builtin_bit_cast(float, x);
  x = __builtin_amdgcn_update_dpp(0, __builtin_bit_cast(int, v), 0x141, 0xF, 0xF, true);
  v += __builtin_bit_cast(float, x);
  x = __builtin_amdgcn_update_dpp(0, __builtin_bit_cast(int, v), 0x140, 0xF, 0xF, true);
  v += __builtin_bit_cast(float, x);
  return v;
}
__device__ __forceinline__ f32x2 unpack2(unsigned u) {
  f32x2 r;
  r[0] = __builtin_bit_cast(float, u << 16);
  r[1] = __builtin_bit_cast(float, u & 0xffff0000u);
  return r;
}
__device__ __forceinline__ unsigned pack2(f32x2 f) {
  bf16x2_t r = __builtin_convertvector(f, bf16x2_t);
  return __builtin_bit_cast(unsigned, r);
}
__device__ __forceinline__ void glds16(const void* gsrc, unsigned lds_dst) {
  unsigned keep;
  asm volatile("s_mov_b32 %0, m0\n\ts_mov_b32 m0, %2\n\ts_nop 0\n\tglobal_load_lds_dwordx4 %1, off\n\ts_mov_b32 m0, %0"
               : "=&s"(keep) : "v"(gsrc), "s"(lds_dst) : "memory");
}
#define WAIT_V(n) asm volatile("s_waitcnt vmcnt(" #n ")" ::: "memory")
#define WAIT_L0 asm volatile("s_waitcnt lgkmcnt(0)" ::: "memory")

__global__ __launch_bounds__(512, 2) void ln_gemm_kernel(const u16* __restrict__ x, const u16* __restrict__ g,
                                                         const u16* __restrict__ b, const u16* __restrict__ w,
                                                         u16* __restrict__ out
) {
  __shared__ __attribute__((aligned(16))) char smem[A_BYTES + NST * W_BYTES];
  char* sA = smem;
  char* sW = smem + A_BYTES;
  const int tid = threadIdx.x, lane = tid & 63;
  const int wid = __builtin_amdgcn_readfirstlane(tid >> 6);
  const int bid = blockIdx.x, xcd = bid & 7, idx = bid >> 3;
  const int cb = idx & 1, rb = (idx >> 1) * 8 + xcd;
  const int row0 = rb * BM, col0 = cb * BN;

  const int rp = lane >> 3, pp = lane & 7;
  const int psw = pp ^ (((rp >> 1) & 3) << 1);
  const int pw = wid & 3;
  const u16* wsrc[5];
#pragma unroll
  for (int i = 0; i < 5; ++i) {
    const int blk = pw + 4 * i;
    const int kb = blk / 5, n64 = blk % 5;
    const int r = rp ^ ((kb & 1) << 2);
    wsrc[i] = w + (size_t)(kb * 8 + r) * ND + col0 + n64 * 64 + psw * 8;
  }
  const unsigned sWaddr = __builtin_amdgcn_readfirstlane((unsigned)(size_t)sW) + pw * 1024;
  auto dma = [&](int s) {
    const unsigned dst = sWaddr + (s & (NST - 1)) * W_BYTES;
    const size_t go = (size_t)s * BK * ND;
#pragma unroll
    for (int i = 0; i < 5; ++i) glds16(wsrc[i] + go, dst + i * 4096);
  };

  const int cl = lane & 15, rq = lane >> 4;
  u32x4 xr[2][5], gr[5], br[5];
#pragma unroll
  for (int h = 0; h < 2; ++h)
#pragma unroll
    for (int i = 0; i < 5; ++i)
      xr[h][i] = *(const u32x4*)(x + (size_t)(row0 + wid * 8 + h * 4 + rq) * KD + (cl + 16 * i) * 8);
#pragma unroll
  for (int i = 0; i < 5; ++i) {
    gr[i] = *(const u32x4*)(g + (cl + 16 * i) * 8);
    br[i] = *(const u32x4*)(b + (cl + 16 * i) * 8);
  }
  if (wid >= 4) { dma(0); dma(1); dma(2); }

#pragma unroll
  for (int h = 0; h < 2; ++h) {
    f32x2 xv[5][4];
    f32x2 s2 = {0.f, 0.f};
#pragma unroll
    for (int i = 0; i < 5; ++i)
#pragma unroll
      for (int e = 0; e < 4; ++e) { const unsigned u = xr[h][i][e]; xv[i][e] = unpack2(u); s2 += xv[i][e]; }
    const float mean = dpp_sum16(s2[0] + s2[1]) * (1.0f / KD);
    const f32x2 m2 = {mean, mean};
    f32x2 q2 = {0.f, 0.f};
#pragma unroll
    for (int i = 0; i < 5; ++i)
#pragma unroll
      for (int e = 0; e < 4; ++e) { xv[i][e] -= m2; q2 += xv[i][e] * xv[i][e]; }
    const float rstd = rsqrtf(dpp_sum16(q2[0] + q2[1]) * (1.0f / KD) + 1e-5f);
    const f32x2 r2 = {rstd, rstd};
    const int r = wid * 8 + h * 4 + rq;
#pragma unroll
    for (int i = 0; i < 5; ++i) {
      u32x4 o;
#pragma unroll
      for (int e = 0; e < 4; ++e) {
        const unsigned gu = gr[i][e], bu = br[i][e];
        f32x2 y = (xv[i][e] * r2) * unpack2(gu) + unpack2(bu);
        o[e] = pack2(y);
      }
      *(u32x4*)(sA + r * 1280 + ((cl ^ (r & 15)) + 16 * i) * 16) = o;
    }
  }

  if (wid >= 4) {
    WAIT_V(10);
    WAIT_L0;
    __builtin_amdgcn_s_barrier();
    dma(3);
#pragma unroll 1
    for (int s = 0; s < NIT - 1; ++s) {
      if (s + 3 < NIT) WAIT_V(10);
      else if (s + 2 < NIT) WAIT_V(5);
      else WAIT_V(0);
      __builtin_amdgcn_s_barrier();
      if (s + 4 < NIT) dma(s + 4);
    }
  } else {
  const int nq = wid;
  const int fg = lane >> 4, fq = (lane & 15) >> 2, fp = lane & 3;
  f32x4 acc[4][5];
#pragma unroll
  for (int m = 0; m < 4; ++m)
#pragma unroll
    for (int n = 0; n < 5; ++n) acc[m][n] = (f32x4){0.f, 0.f, 0.f, 0.f};
  const int ar = (lane & 15);
  int boff[5][2];
#pragma unroll
  for (int nf = 0; nf < 5; ++nf) {
    const int c16 = nq * 5 + nf, n64 = c16 >> 2, j = c16 & 3;
#pragma unroll
    for (int t = 0; t < 2; ++t) {
      const int rpp = (4 * t + fq) ^ ((fg & 1) << 2);
      const int piece = (2 * j + (fp >> 1)) ^ (((rpp >> 1) & 3) << 1);
      boff[nf][t] = A_BYTES + (fg * 5 + n64) * 1024 + rpp * 128 + piece * 16 + (fp & 1) * 8;
    }
  }
  struct Frags { bf16x8 a[4]; s16x4 lo[5], hi[5]; };
  int aoff[4];
#pragma unroll
  for (int j = 0; j < 4; ++j) aoff[j] = ar * 1280 + (((4 * j + fg) ^ (ar & 15)) * 16);
  auto rd = [&](Frags& F, int s) {
    const char* wb = smem + (s & (NST - 1)) * W_BYTES;
    const char* ab = sA + aoff[s & 3] + (s >> 2) * 256;
#pragma unroll
    for (int m = 0; m < 4; ++m) F.a[m] = *(const bf16x8*)(ab + m * 16 * 1280);
#pragma unroll
    for (int nf = 0; nf < 5; ++nf) {
      F.lo[nf] = __builtin_bit_cast(s16x4, __builtin_amdgcn_ds_read_tr16_b64_v4i16(
          (__attribute__((address_space(3))) v4i16_t*)(wb + boff[nf][0])));
      F.hi[nf] = __builtin_bit_cast(s16x4, __builtin_amdgcn_ds_read_tr16_b64_v4i16(
          (__attribute__((address_space(3))) v4i16_t*)(wb + boff[nf][1])));
    }
  };
  auto mm = [&](const Frags& F) {
#pragma unroll
    for (int nf = 0; nf < 5; ++nf) {
      bf16x8 bfr = {F.lo[nf][0], F.lo[nf][1], F.lo[nf][2], F.lo[nf][3], F.hi[nf][0], F.hi[nf][1], F.hi[nf][2], F.hi[nf][3]};
#pragma unroll
      for (int m = 0; m < 4; ++m)
        acc[m][nf] = __builtin_amdgcn_mfma_f32_16x16x32_bf16(bfr, F.a[m], acc[m][nf], 0, 0, 0);
    }
  };
  Frags F[2];
  WAIT_L0;
  __builtin_amdgcn_s_barrier();
  rd(F[0], 0);
#pragma unroll
  for (int s = 0; s < NIT; ++s) {
    if (s + 1 < NIT) {
      WAIT_L0;
      __builtin_amdgcn_s_barrier();
      rd(F[(s + 1) & 1], s + 1);
    }
    mm(F[s & 1]);
    if (s + 1 < NIT) {
#pragma unroll
      for (int i = 0; i < 14; ++i) {
        __builtin_amdgcn_sched_group_barrier(0x100, 1, 0);
        __builtin_amdgcn_sched_group_barrier(0x8, 1, 0);
      }
      __builtin_amdgcn_sched_group_barrier(0x8, 6, 0);
    }
    __builtin_amdgcn_sched_barrier(0);
  }

#pragma unroll
  for (int m = 0; m < 4; ++m)
#pragma unroll
    for (int n = 0; n < 5; ++n) {
      u32x2 o;
      o[0] = pack2((f32x2){acc[m][n][0], acc[m][n][1]});
      o[1] = pack2((f32x2){acc[m][n][2], acc[m][n][3]});
      *(u32x2*)(out + (size_t)(row0 + m * 16 + (lane & 15)) * ND + col0 + nq * 80 + n * 16 + fg * 4) = o;
    }
  }
}

extern "C" void kernel_launch(void* const* d_in, const int* in_sizes, int n_in,
                              void* d_out, int out_size, void* d_ws, size_t ws_size,
                              hipStream_t stream) {
  const u16* x = (const u16*)d_in[0];
  const u16* g = (const u16*)d_in[1];
  const u16* b = (const u16*)d_in[2];
  const u16* w = (const u16*)d_in[3];
  u16* out = (u16*)d_out;
  const int M = in_sizes[0] / KD;
  ln_gemm_kernel<<<dim3((M / BM) * (ND / BN)), dim3(512), 0, stream>>>(x, g, b, w, out
  );
}
```

```cpp
#include <hip/hip_runtime.h>
#include <hip/hip_bf16.h>
#include <stdint.h>

typedef __attribute__((ext_vector_type(8))) short bf16x8;
typedef __attribute__((ext_vector_type(4))) short s16x4;
typedef __attribute__((ext_vector_type(4))) float f32x4;
typedef __attribute__((ext_vector_type(2))) float f32x2;
typedef __attribute__((ext_vector_type(4))) unsigned u32x4;
typedef __attribute__((ext_vector_type(2))) unsigned u32x2;
typedef __attribute__((ext_vector_type(2))) __bf16 bf16x2_t;
typedef __attribute__((ext_vector_type(4))) short v4i16_t;
typedef unsigned short u16;

constexpr int KD = 640, ND = 640;
constexpr int BM = 64, BN = 320, BK = 32, NST = 4, NIT = KD / BK;
constexpr int A_BYTES = BM * KD * 2;
constexpr int W_BYTES = BK * BN * 2;

__device__ __forceinline__ float dpp_sum16(float v) {
  int x;
  x = __builtin_amdgcn_update_dpp(0, __builtin_bit_cast(int, v), 0xB1, 0xF, 0xF, true);
  v += __builtin_bit_cast(float, x);
  x = __builtin_amdgcn_update_dpp(0, __builtin_bit_cast(int, v), 0x4E, 0xF, 0xF, true);
  v += __builtin_bit_cast(float, x);
  x = __builtin_amdgcn_update_dpp(0, __builtin_bit_cast(int, v), 0x141, 0xF, 0xF, true);
  v += __builtin_bit_cast(float, x);
  x = __builtin_amdgcn_update_dpp(0, __builtin_bit_cast(int, v), 0x140, 0xF, 0xF, true);
  v += __builtin_bit_cast(float, x);
  return v;
}
__device__ __forceinline__ f32x2 unpack2(unsigned u) {
  f32x2 r;
  r[0] = __builtin_bit_cast(float, u << 16);
  r[1] = __builtin_bit_cast(float, u & 0xffff0000u);
  return r;
}
__device__ __forceinline__ unsigned pack2(f32x2 f) {
  bf16x2_t r = __builtin_convertvector(f, bf16x2_t);
  return __builtin_bit_cast(unsigned, r);
}
__device__ __forceinline__ void glds16(const void* gsrc, unsigned lds_dst) {
  unsigned keep;
  asm volatile("s_mov_b32 %0, m0\n\ts_mov_b32 m0, %2\n\ts_nop 0\n\tglobal_load_lds_dwordx4 %1, off\n\ts_mov_b32 m0, %0"
               : "=&s"(keep) : "v"(gsrc), "s"(lds_dst) : "memory");
}
#define WAIT_V(n) asm volatile("s_waitcnt vmcnt(" #n ")" ::: "memory")
#define WAIT_L0 asm volatile("s_waitcnt lgkmcnt(0)" ::: "memory")

__global__ __launch_bounds__(512, 2) void ln_gemm_kernel(const u16* __restrict__ x, const u16* __restrict__ g,
                                                         const u16* __restrict__ b, const u16* __restrict__ w,
                                                         u16* __restrict__ out
) {
  __shared__ __attribute__((aligned(16))) char smem[A_BYTES + NST * W_BYTES];
  char* sA = smem;
  char* sW = smem + A_BYTES;
  const int tid = threadIdx.x, lane = tid & 63;
  const int wid = __builtin_amdgcn_readfirstlane(tid >> 6);
  const int bid = blockIdx.x, xcd = bid & 7, idx = bid >> 3;
  const int cb = idx & 1, rb = (idx >> 1) * 8 + xcd;
  const int row0 = rb * BM, col0 = cb * BN;

  const int rp = lane >> 3, pp = lane & 7;
  const int psw = pp ^ (((rp >> 1) & 3) << 1);
  const int pw = wid & 3;
  const u16* wsrc[5];
#pragma unroll
  for (int i = 0; i < 5; ++i) {
    const int blk = pw + 4 * i;
    const int kb = blk / 5, n64 = blk % 5;
    const int r = rp ^ ((kb & 1) << 2);
    wsrc[i] = w + (size_t)(kb * 8 + r) * ND + col0 + n64 * 64 + psw * 8;
  }
  const unsigned sWaddr = __builtin_amdgcn_readfirstlane((unsigned)(size_t)sW) + pw * 1024;
  auto dma = [&](int s) {
    const unsigned dst = sWaddr + (s & (NST - 1)) * W_BYTES;
    const size_t go = (size_t)s * BK * ND;
#pragma unroll
    for (int i = 0; i < 5; ++i) glds16(wsrc[i] + go, dst + i * 4096);
  };

  u32x4 dummy = {0, 0, 0, 0};
  if (wid >= 4) {
    const char* wp = (const char*)w + (size_t)idx * 25600 + pw * 6400 + lane * 16;
#pragma unroll
    for (int i = 0; i < 7; ++i) {
      const int o = (i < 6) ? i * 1024 : 6400 - 1024;
      asm volatile("global_load_dwordx4 %0, %1, off" : "+v"(dummy) : "v"(wp + o) : "memory");
    }
  }
  const int cl = lane & 15, rq = lane >> 4;
  u32x4 xr[2][5], gr[5], br[5];
#pragma unroll
  for (int h = 0; h < 2; ++h)
#pragma unroll
    for (int i = 0; i < 5; ++i)
      xr[h][i] = *(const u32x4*)(x + (size_t)(row0 + wid * 8 + h * 4 + rq) * KD + (cl + 16 * i) * 8);
#pragma unroll
  for (int i = 0; i < 5; ++i) {
    gr[i] = *(const u32x4*)(g + (cl + 16 * i) * 8);
    br[i] = *(const u32x4*)(b + (cl + 16 * i) * 8);
  }
  if (wid >= 4) { dma(0); dma(1); dma(2); }

#pragma unroll
  for (int h = 0; h < 2; ++h) {
    f32x2 xv[5][4];
    f32x2 s2 = {0.f, 0.f};
#pragma unroll
    for (int i = 0; i < 5; ++i)
#pragma unroll
      for (int e = 0; e < 4; ++e) { const unsigned u = xr[h][i][e]; xv[i][e] = unpack2(u); s2 += xv[i][e]; }
    const float mean = dpp_sum16(s2[0] + s2[1]) * (1.0f / KD);
    const f32x2 m2 = {mean, mean};
    f32x2 q2 = {0.f, 0.f};
#pragma unroll
    for (int i = 0; i < 5; ++i)
#pragma unroll
      for (int e = 0; e < 4; ++e) { xv[i][e] -= m2; q2 += xv[i][e] * xv[i][e]; }
    const float rstd = rsqrtf(dpp_sum16(q2[0] + q2[1]) * (1.0f / KD) + 1e-5f);
    const f32x2 r2 = {rstd, rstd};
    const int r = wid * 8 + h * 4 + rq;
#pragma unroll
    for (int i = 0; i < 5; ++i) {
      u32x4 o;
#pragma unroll
      for (int e = 0; e < 4; ++e) {
        const unsigned gu = gr[i][e], bu = br[i][e];
        f32x2 y = (xv[i][e] * r2) * unpack2(gu) + unpack2(bu);
        o[e] = pack2(y);
      }
      *(u32x4*)(sA + r * 1280 + ((cl ^ (r & 15)) + 16 * i) * 16) = o;
    }
  }

  asm volatile("" :: "v"(dummy));
  if (wid >= 4) {
    WAIT_V(10);
    WAIT_L0;
    __builtin_amdgcn_s_barrier();
    dma(3);
#pragma unroll 1
    for (int s = 0; s < NIT - 1; ++s) {
      if (s + 3 < NIT) WAIT_V(10);
      else if (s + 2 < NIT) WAIT_V(5);
      else WAIT_V(0);
      __builtin_amdgcn_s_barrier();
      if (s + 4 < NIT) dma(s + 4);
    }
  } else {
  const int nq = wid;
  const int fg = lane >> 4, fq = (lane & 15) >> 2, fp = lane & 3;
  f32x4 acc[4][5];
#pragma unroll
  for (int m = 0; m < 4; ++m)
#pragma unroll
    for (int n = 0; n < 5; ++n) acc[m][n] = (f32x4){0.f, 0.f, 0.f, 0.f};
  const int ar = (lane & 15);
  int boff[5][2];
#pragma unroll
  for (int nf = 0; nf < 5; ++nf) {
    const int c16 = nq * 5 + nf, n64 = c16 >> 2, j = c16 & 3;
#pragma unroll
    for (int t = 0; t < 2; ++t) {
      const int rpp = (4 * t + fq) ^ ((fg & 1) << 2);
      const int piece = (2 * j + (fp >> 1)) ^ (((rpp >> 1) & 3) << 1);
      boff[nf][t] = A_BYTES + (fg * 5 + n64) * 1024 + rpp * 128 + piece * 16 + (fp & 1) * 8;
    }
  }
  struct Frags { bf16x8 a[4]; s16x4 lo[5], hi[5]; };
  int aoff[4];
#pragma unroll
  for (int j = 0; j < 4; ++j) aoff[j] = ar * 1280 + (((4 * j + fg) ^ (ar & 15)) * 16);
  auto rd = [&](Frags& F, int s) {
    const char* wb = smem + (s & (NST - 1)) * W_BYTES;
    const char* ab = sA + aoff[s & 3] + (s >> 2) * 256;
#pragma unroll
    for (int m = 0; m < 4; ++m) F.a[m] = *(const bf16x8*)(ab + m * 16 * 1280);
#pragma unroll
    for (int nf = 0; nf < 5; ++nf) {
      F.lo[nf] = __builtin_bit_cast(s16x4, __builtin_amdgcn_ds_read_tr16_b64_v4i16(
          (__attribute__((address_space(3))) v4i16_t*)(wb + boff[nf][0])));
      F.hi[nf] = __builtin_bit_cast(s16x4, __builtin_amdgcn_ds_read_tr16_b64_v4i16(
          (__attribute__((address_space(3))) v4i16_t*)(wb + boff[nf][1])));
    }
  };
  auto mm = [&](const Frags& F) {
#pragma unroll
    for (int nf = 0; nf < 5; ++nf) {
      bf16x8 bfr = {F.lo[nf][0], F.lo[nf][1], F.lo[nf][2], F.lo[nf][3], F.hi[nf][0], F.hi[nf][1], F.hi[nf][2], F.hi[nf][3]};
#pragma unroll
      for (int m = 0; m < 4; ++m)
        acc[m][nf] = __builtin_amdgcn_mfma_f32_16x16x32_bf16(bfr, F.a[m], acc[m][nf], 0, 0, 0);
    }
  };
  Frags F[2];
  WAIT_L0;
  __builtin_amdgcn_s_barrier();
  rd(F[0], 0);
#pragma unroll
  for (int s = 0; s < NIT; ++s) {
    if (s + 1 < NIT) {
      WAIT_L0;
      __builtin_amdgcn_s_barrier();
      rd(F[(s + 1) & 1], s + 1);
    }
    mm(F[s & 1]);
    if (s + 1 < NIT) {
#pragma unroll
      for (int i = 0; i < 14; ++i) {
        __builtin_amdgcn_sched_group_barrier(0x100, 1, 0);
        __builtin_amdgcn_sched_group_barrier(0x8, 1, 0);
      }
      __builtin_amdgcn_sched_group_barrier(0x8, 6, 0);
    }
    __builtin_amdgcn_sched_barrier(0);
  }

#pragma unroll
  for (int m = 0; m < 4; ++m)
#pragma unroll
    for (int n = 0; n < 5; ++n) {
      u32x2 o;
      o[0] = pack2((f32x2){acc[m][n][0], acc[m][n][1]});
      o[1] = pack2((f32x2){acc[m][n][2], acc[m][n][3]});
      *(u32x2*)(out + (size_t)(row0 + m * 16 + (lane & 15)) * ND + col0 + nq * 80 + n * 16 + fg * 4) = o;
    }
  }
}

extern "C" void kernel_launch(void* const* d_in, const int* in_sizes, int n_in,
                              void* d_out, int out_size, void* d_ws, size_t ws_size,
                              hipStream_t stream) {
  const u16* x = (const u16*)d_in[0];
  const u16* g = (const u16*)d_in[1];
  const u16* b = (const u16*)d_in[2];
  const u16* w = (const u16*)d_in[3];
  u16* out = (u16*)d_out;
  const int M = in_sizes[0] / KD;
  ln_gemm_kernel<<<dim3((M / BM) * (ND / BN)), dim3(512), 0, stream>>>(x, g, b, w, out
  );
}
```

```cpp
#include <hip/hip_runtime.h>
#include <hip/hip_bf16.h>
#include <stdint.h>

typedef __attribute__((ext_vector_type(8))) short bf16x8;
typedef __attribute__((ext_vector_type(4))) short s16x4;
typedef __attribute__((ext_vector_type(4))) float f32x4;
typedef __attribute__((ext_vector_type(2))) float f32x2;
typedef __attribute__((ext_vector_type(4))) unsigned u32x4;
typedef __attribute__((ext_vector_type(2))) unsigned u32x2;
typedef __attribute__((ext_vector_type(2))) __bf16 bf16x2_t;
typedef __attribute__((ext_vector_type(4))) short v4i16_t;
typedef unsigned short u16;

constexpr int KD = 640, ND = 640;
constexpr int BM = 64, BN = 320, BK = 32, NST = 4, NIT = KD / BK;
constexpr int A_BYTES = BM * KD * 2;
constexpr int W_BYTES = BK * BN * 2;

__device__ __forceinline__ float dpp_sum16(float v) {
  int x;
  x = __builtin_amdgcn_update_dpp(0, __builtin_bit_cast(int, v), 0xB1, 0xF, 0xF, true);
  v += __builtin_bit_cast(float, x);
  x = __builtin_amdgcn_update_dpp(0, __builtin_bit_cast(int, v), 0x4E, 0xF, 0xF, true);
  v += __builtin_bit_cast(float, x);
  x = __builtin_amdgcn_update_dpp(0, __builtin_bit_cast(int, v), 0x141, 0xF, 0xF, true);
  v += __builtin_bit_cast(float, x);
  x = __builtin_amdgcn_update_dpp(0, __builtin_bit_cast(int, v), 0x140, 0xF, 0xF, true);
  v += __builtin_bit_cast(float, x);
  return v;
}
__device__ __forceinline__ f32x2 unpack2(unsigned u) {
  f32x2 r;
  r[0] = __builtin_bit_cast(float, u << 16);
  r[1] = __builtin_bit_cast(float, u & 0xffff0000u);
  return r;
}
__device__ __forceinline__ unsigned pack2(f32x2 f) {
  bf16x2_t r = __builtin_convertvector(f, bf16x2_t);
  return __builtin_bit_cast(unsigned, r);
}
__device__ __forceinline__ void glds16(const void* gsrc, unsigned lds_dst) {
  unsigned keep;
  asm volatile("s_mov_b32 %0, m0\n\ts_mov_b32 m0, %2\n\ts_nop 0\n\tglobal_load_lds_dwordx4 %1, off\n\ts_mov_b32 m0, %0"
               : "=&s"(keep) : "v"(gsrc), "s"(lds_dst) : "memory");
}
#define WAIT_V(n) asm volatile("s_waitcnt vmcnt(" #n ")" ::: "memory")
#define WAIT_L0 asm volatile("s_waitcnt lgkmcnt(0)" ::: "memory")

__global__ __launch_bounds__(512, 2) void ln_gemm_kernel(const u16* __restrict__ x, const u16* __restrict__ g,
                                                         const u16* __restrict__ b, const u16* __restrict__ w,
                                                         u16* __restrict__ out
) {
  __shared__ __attribute__((aligned(16))) char smem[A_BYTES + NST * W_BYTES];
  char* sA = smem;
  char* sW = smem + A_BYTES;
  const int tid = threadIdx.x, lane = tid & 63;
  const int wid = __builtin_amdgcn_readfirstlane(tid >> 6);
  const int bid = blockIdx.x, xcd = bid & 7, idx = bid >> 3;
  const int cb = idx & 1, rb = (idx >> 1) * 8 + xcd;
  const int row0 = rb * BM, col0 = cb * BN;

  const int rp = lane >> 3, pp = lane & 7;
  const int psw = pp ^ (((rp >> 1) & 3) << 1);
  const int pw = wid & 3;
  const u16* wsrc[5];
#pragma unroll
  for (int i = 0; i < 5; ++i) {
    const int blk = pw + 4 * i;
    const int kb = blk / 5, n64 = blk % 5;
    const int r = rp ^ ((kb & 1) << 2);
    wsrc[i] = w + (size_t)(kb * 8 + r) * ND + col0 + n64 * 64 + psw * 8;
  }
  const unsigned sWaddr = __builtin_amdgcn_readfirstlane((unsigned)(size_t)sW) + pw * 1024;
  auto dma = [&](int s) {
    const unsigned dst = sWaddr + (s & (NST - 1)) * W_BYTES;
    const size_t go = (size_t)s * BK * ND;
#pragma unroll
    for (int i = 0; i < 5; ++i) glds16(wsrc[i] + go, dst + i * 4096);
  };

  u32x4 dummy = {0, 0, 0, 0};
  if (wid >= 4) {
    const char* wp = (const char*)w + (size_t)idx * 25600 + pw * 6400 + lane * 16;
#pragma unroll
    for (int i = 0; i < 7; ++i) {
      const int o = (i < 6) ? i * 1024 : 6400 - 1024;
      asm volatile("global_load_dwordx4 %0, %1, off" : "+v"(dummy) : "v"(wp + o) : "memory");
    }
  }
  const int cl = lane & 15, rq = lane >> 4;
  u32x4 xr[2][5], gr[5], br[5];
#pragma unroll
  for (int h = 0; h < 2; ++h)
#pragma unroll
    for (int i = 0; i < 5; ++i)
      xr[h][i] = *(const u32x4*)(x + (size_t)(row0 + wid * 8 + h * 4 + rq) * KD + (cl + 16 * i) * 8);
#pragma unroll
  for (int i = 0; i < 5; ++i) {
    gr[i] = *(const u32x4*)(g + (cl + 16 * i) * 8);
    br[i] = *(const u32x4*)(b + (cl + 16 * i) * 8);
  }
  if (wid >= 4) { dma(0); dma(1); dma(2); }

#pragma unroll
  for (int h = 0; h < 2; ++h) {
    f32x2 xv[5][4];
    f32x2 s2 = {0.f, 0.f};
#pragma unroll
    for (int i = 0; i < 5; ++i)
#pragma unroll
      for (int e = 0; e < 4; ++e) { const unsigned u = xr[h][i][e]; xv[i][e] = unpack2(u); s2 += xv[i][e]; }
    const float mean = dpp_sum16(s2[0] + s2[1]) * (1.0f / KD);
    const f32x2 m2 = {mean, mean};
    f32x2 q2 = {0.f, 0.f};
#pragma unroll
    for (int i = 0; i < 5; ++i)
#pragma unroll
      for (int e = 0; e < 4; ++e) { xv[i][e] -= m2; q2 += xv[i][e] * xv[i][e]; }
    const float rstd = rsqrtf(dpp_sum16(q2[0] + q2[1]) * (1.0f / KD) + 1e-5f);
    const f32x2 r2 = {rstd, rstd};
    const int r = wid * 8 + h * 4 + rq;
#pragma unroll
    for (int i = 0; i < 5; ++i) {
      u32x4 o;
#pragma unroll
      for (int e = 0; e < 4; ++e) {
        const unsigned gu = gr[i][e], bu = br[i][e];
        f32x2 y = (xv[i][e] * r2) * unpack2(gu) + unpack2(bu);
        o[e] = pack2(y);
      }
      *(u32x4*)(sA + r * 1280 + ((cl ^ (r & 15)) + 16 * i) * 16) = o;
    }
  }

  asm volatile("" :: "v"(dummy));
  if (wid >= 4) {
    WAIT_V(10);
    WAIT_L0;
    __builtin_amdgcn_s_barrier();
    dma(3);
#pragma unroll 1
    for (int s = 0; s < NIT; ++s) {
      if (s + 3 < NIT) WAIT_V(10);
      else if (s + 2 < NIT) WAIT_V(5);
      else WAIT_V(0);
      __builtin_amdgcn_s_barrier();
      if (s + 4 < NIT) dma(s + 4);
    }
  } else {
  const int nq = wid;
  const int fg = lane >> 4, fq = (lane & 15) >> 2, fp = lane & 3;
  f32x4 acc[4][5];
#pragma unroll
  for (int m = 0; m < 4; ++m)
#pragma unroll
    for (int n = 0; n < 5; ++n) acc[m][n] = (f32x4){0.f, 0.f, 0.f, 0.f};
  const int ar = (lane & 15);
  int boff[5][2];
#pragma unroll
  for (int nf = 0; nf < 5; ++nf) {
    const int c16 = nq * 5 + nf, n64 = c16 >> 2, j = c16 & 3;
#pragma unroll
    for (int t = 0; t < 2; ++t) {
      const int rpp = (4 * t + fq) ^ ((fg & 1) << 2);
      const int piece = (2 * j + (fp >> 1)) ^ (((rpp >> 1) & 3) << 1);
      boff[nf][t] = A_BYTES + (fg * 5 + n64) * 1024 + rpp * 128 + piece * 16 + (fp & 1) * 8;
    }
  }
  struct Frags { bf16x8 a[4]; s16x4 lo[5], hi[5]; };
  int aoff[4];
#pragma unroll
  for (int j = 0; j < 4; ++j) aoff[j] = ar * 1280 + (((4 * j + fg) ^ (ar & 15)) * 16);
  auto rd = [&](Frags& F, int j, int goff) {
    const char* wb = smem + j * W_BYTES;
    const char* ab = sA + aoff[j] + goff;
#pragma unroll
    for (int m = 0; m < 4; ++m) F.a[m] = *(const bf16x8*)(ab + m * 16 * 1280);
#pragma unroll
    for (int nf = 0; nf < 5; ++nf) {
      F.lo[nf] = __builtin_bit_cast(s16x4, __builtin_amdgcn_ds_read_tr16_b64_v4i16(
          (__attribute__((address_space(3))) v4i16_t*)(wb + boff[nf][0])));
      F.hi[nf] = __builtin_bit_cast(s16x4, __builtin_amdgcn_ds_read_tr16_b64_v4i16(
          (__attribute__((address_space(3))) v4i16_t*)(wb + boff[nf][1])));
    }
  };
  auto mm = [&](const Frags& F) {
#pragma unroll
    for (int nf = 0; nf < 5; ++nf) {
      bf16x8 bfr = {F.lo[nf][0], F.lo[nf][1], F.lo[nf][2], F.lo[nf][3], F.hi[nf][0], F.hi[nf][1], F.hi[nf][2], F.hi[nf][3]};
#pragma unroll
      for (int m = 0; m < 4; ++m)
        acc[m][nf] = __builtin_amdgcn_mfma_f32_16x16x32_bf16(bfr, F.a[m], acc[m][nf], 0, 0, 0);
    }
  };
  Frags F[2];
  WAIT_L0;
  __builtin_amdgcn_s_barrier();
  rd(F[0], 0, 0);
#pragma unroll 1
  for (int gI = 0; gI < NIT / 4; ++gI) {
#pragma unroll
    for (int j = 0; j < 4; ++j) {
      WAIT_L0;
      __builtin_amdgcn_s_barrier();
      rd(F[(j + 1) & 1], (j + 1) & 3, (gI + ((j + 1) >> 2)) * 256);
      mm(F[j & 1]);
#pragma unroll
      for (int i = 0; i < 14; ++i) {
        __builtin_amdgcn_sched_group_barrier(0x100, 1, 0);
        __builtin_amdgcn_sched_group_barrier(0x8, 1, 0);
      }
      __builtin_amdgcn_sched_group_barrier(0x8, 6, 0);
      __builtin_amdgcn_sched_barrier(0);
    }
  }

#pragma unroll
  for (int m = 0; m < 4; ++m)
#pragma unroll
    for (int n = 0; n < 5; ++n) {
      u32x2 o;
      o[0] = pack2((f32x2){acc[m][n][0], acc[m][n][1]});
      o[1] = pack2((f32x2){acc[m][n][2], acc[m][n][3]});
      *(u32x2*)(out + (size_t)(row0 + m * 16 + (lane & 15)) * ND + col0 + nq * 80 + n * 16 + fg * 4) = o;
    }
  }
}

extern "C" void kernel_launch(void* const* d_in, const int* in_sizes, int n_in,
                              void* d_out, int out_size, void* d_ws, size_t ws_size,
                              hipStream_t stream) {
  const u16* x = (const u16*)d_in[0];
  const u16* g = (const u16*)d_in[1];
  const u16* b = (const u16*)d_in[2];
  const u16* w = (const u16*)d_in[3];
  u16* out = (u16*)d_out;
  const int M = in_sizes[0] / KD;
  ln_gemm_kernel<<<dim3((M / BM) * (ND / BN)), dim3(512), 0, stream>>>(x, g, b, w, out
  );
}
```

```cpp
#include <hip/hip_runtime.h>
#include <hip/hip_bf16.h>
#include <stdint.h>

typedef __attribute__((ext_vector_type(8))) short bf16x8;
typedef __attribute__((ext_vector_type(4))) short s16x4;
typedef __attribute__((ext_vector_type(4))) float f32x4;
typedef __attribute__((ext_vector_type(2))) float f32x2;
typedef __attribute__((ext_vector_type(4))) unsigned u32x4;
typedef __attribute__((ext_vector_type(2))) unsigned u32x2;
typedef __attribute__((ext_vector_type(2))) __bf16 bf16x2_t;
typedef __attribute__((ext_vector_type(4))) short v4i16_t;
typedef unsigned short u16;

constexpr int KD = 640, ND = 640;
constexpr int BM = 64, BN = 320, BK = 32, NST = 4, NIT = KD / BK;
constexpr int A_BYTES = BM * KD * 2;
constexpr int W_BYTES = BK * BN * 2;

__device__ __forceinline__ float dpp_sum16(float v) {
  int x;
  x = __builtin_amdgcn_update_dpp(0, __builtin_bit_cast(int, v), 0xB1, 0xF, 0xF, true);
  v += __builtin_bit_cast(float, x);
  x = __builtin_amdgcn_update_dpp(0, __builtin_bit_cast(int, v), 0x4E, 0xF, 0xF, true);
  v += __builtin_bit_cast(float, x);
  x = __builtin_amdgcn_update_dpp(0, __builtin_bit_cast(int, v), 0x141, 0xF, 0xF, true);
  v += __builtin_bit_cast(float, x);
  x = __builtin_amdgcn_update_dpp(0, __builtin_bit_cast(int, v), 0x140, 0xF, 0xF, true);
  v += __builtin_bit_cast(float, x);
  return v;
}
__device__ __forceinline__ f32x2 unpack2(unsigned u) {
  f32x2 r;
  r[0] = __builtin_bit_cast(float, u << 16);
  r[1] = __builtin_bit_cast(float, u & 0xffff0000u);
  return r;
}
__device__ __forceinline__ unsigned pack2(f32x2 f) {
  bf16x2_t r = __builtin_convertvector(f, bf16x2_t);
  return __builtin_bit_cast(unsigned, r);
}
__device__ __forceinline__ void glds16(const void* gsrc, unsigned lds_dst) {
  unsigned keep;
  asm volatile("s_mov_b32 %0, m0\n\ts_mov_b32 m0, %2\n\ts_nop 0\n\tglobal_load_lds_dwordx4 %1, off\n\ts_mov_b32 m0, %0"
               : "=&s"(keep) : "v"(gsrc), "s"(lds_dst) : "memory");
}
#define WAIT_V(n) asm volatile("s_waitcnt vmcnt(" #n ")" ::: "memory")
#define WAIT_L0 asm volatile("s_waitcnt lgkmcnt(0)" ::: "memory")

__global__ __launch_bounds__(512, 2) void ln_gemm_kernel(const u16* __restrict__ x, const u16* __restrict__ g,
                                                         const u16* __restrict__ b, const u16* __restrict__ w,
                                                         u16* __restrict__ out
) {
  __shared__ __attribute__((aligned(16))) char smem[A_BYTES + NST * W_BYTES];
  char* sA = smem;
  char* sW = smem + A_BYTES;
  const int tid = threadIdx.x, lane = tid & 63;
  const int wid = __builtin_amdgcn_readfirstlane(tid >> 6);
  const int bid = blockIdx.x, xcd = bid & 7, idx = bid >> 3;
  const int cb = idx & 1, rb = (idx >> 1) * 8 + xcd;
  const int row0 = rb * BM, col0 = cb * BN;

  const int rp = lane >> 3, pp = lane & 7;
  const int psw = pp ^ (((rp >> 1) & 3) << 1);
  const int pw = wid & 3;
  const u16* wsrc[5];
#pragma unroll
  for (int i = 0; i < 5; ++i) {
    const int blk = pw + 4 * i;
    const int kb = blk / 5, n64 = blk % 5;
    const int r = rp ^ ((kb & 1) << 2);
    wsrc[i] = w + (size_t)(kb * 8 + r) * ND + col0 + n64 * 64 + psw * 8;
  }
  const unsigned sWaddr = __builtin_amdgcn_readfirstlane((unsigned)(size_t)sW) + pw * 1024;
  auto dma = [&](int s) {
    const unsigned dst = sWaddr + (s & (NST - 1)) * W_BYTES;
    const size_t go = (size_t)s * BK * ND;
#pragma unroll
    for (int i = 0; i < 5; ++i) glds16(wsrc[i] + go, dst + i * 4096);
  };

  u32x4 dummy = {0, 0, 0, 0};
  if (wid >= 4) {
    const char* wp = (const char*)w + (size_t)idx * 25600 + pw * 6400 + lane * 16;
#pragma unroll
    for (int i = 0; i < 7; ++i) {
      const int o = (i < 6) ? i * 1024 : 6400 - 1024;
      asm volatile("global_load_dwordx4 %0, %1, off" : "+v"(dummy) : "v"(wp + o) : "memory");
    }
  }
  const int cl = lane & 15, rq = lane >> 4;
  u32x4 xr[2][5], gr[5], br[5];
#pragma unroll
  for (int h = 0; h < 2; ++h)
#pragma unroll
    for (int i = 0; i < 5; ++i)
      xr[h][i] = *(const u32x4*)(x + (size_t)(row0 + wid * 8 + h * 4 + rq) * KD + (cl + 16 * i) * 8);
#pragma unroll
  for (int i = 0; i < 5; ++i) {
    gr[i] = *(const u32x4*)(g + (cl + 16 * i) * 8);
    br[i] = *(const u32x4*)(b + (cl + 16 * i) * 8);
  }
  if (wid >= 4) { dma(0); dma(1); dma(2); }

#pragma unroll
  for (int h = 0; h < 2; ++h) {
    f32x2 xv[5][4];
    f32x2 s2 = {0.f, 0.f};
#pragma unroll
    for (int i = 0; i < 5; ++i)
#pragma unroll
      for (int e = 0; e < 4; ++e) { const unsigned u = xr[h][i][e]; xv[i][e] = unpack2(u); s2 += xv[i][e]; }
    const float mean = dpp_sum16(s2[0] + s2[1]) * (1.0f / KD);
    const f32x2 m2 = {mean, mean};
    f32x2 q2 = {0.f, 0.f};
#pragma unroll
    for (int i = 0; i < 5; ++i)
#pragma unroll
      for (int e = 0; e < 4; ++e) { xv[i][e] -= m2; q2 += xv[i][e] * xv[i][e]; }
    const float rstd = rsqrtf(dpp_sum16(q2[0] + q2[1]) * (1.0f / KD) + 1e-5f);
    const f32x2 r2 = {rstd, rstd};
    const int r = wid * 8 + h * 4 + rq;
#pragma unroll
    for (int i = 0; i < 5; ++i) {
      u32x4 o;
#pragma unroll
      for (int e = 0; e < 4; ++e) {
        const unsigned gu = gr[i][e], bu = br[i][e];
        f32x2 y = (xv[i][e] * r2) * unpack2(gu) + unpack2(bu);
        o[e] = pack2(y);
      }
      *(u32x4*)(sA + r * 1280 + ((cl ^ (r & 15)) + 16 * i) * 16) = o;
    }
  }

  asm volatile("" :: "v"(dummy));
  if (wid >= 4) {
    WAIT_V(10);
    WAIT_L0;
    __builtin_amdgcn_s_barrier();
    dma(3);
#pragma unroll 1
    for (int s = 0; s < NIT; ++s) {
      if (s + 3 < NIT) WAIT_V(10);
      else if (s + 2 < NIT) WAIT_V(5);
      else WAIT_V(0);
      __builtin_amdgcn_s_barrier();
      if (s + 4 < NIT) dma(s + 4);
    }
  } else {
  const int nq = wid;
  const int fg = lane >> 4, fq = (lane & 15) >> 2, fp = lane & 3;
  f32x4 acc[4][5];
#pragma unroll
  for (int m = 0; m < 4; ++m)
#pragma unroll
    for (int n = 0; n < 5; ++n) acc[m][n] = (f32x4){0.f, 0.f, 0.f, 0.f};
  const int ar = (lane & 15);
  int boff[5][2];
#pragma unroll
  for (int nf = 0; nf < 5; ++nf) {
    const int c16 = nq * 5 + nf, n64 = c16 >> 2, j = c16 & 3;
#pragma unroll
    for (int t = 0; t < 2; ++t) {
      const int rpp = (4 * t + fq) ^ ((fg & 1) << 2);
      const int piece = (2 * j + (fp >> 1)) ^ (((rpp >> 1) & 3) << 1);
      boff[nf][t] = A_BYTES + (fg * 5 + n64) * 1024 + rpp * 128 + piece * 16 + (fp & 1) * 8;
    }
  }
  struct Frags { bf16x8 a[4]; s16x4 lo[5], hi[5]; };
  int aoff[4];
#pragma unroll
  for (int j = 0; j < 4; ++j) aoff[j] = ar * 1280 + (((4 * j + fg) ^ (ar & 15)) * 16);
  auto rd = [&](Frags& F, int j, int goff) {
    const char* wb = smem + j * W_BYTES;
    const char* ab = sA + aoff[j] + goff;
#pragma unroll
    for (int m = 0; m < 4; ++m) F.a[m] = *(const bf16x8*)(ab + m * 16 * 1280);
#pragma unroll
    for (int nf = 0; nf < 5; ++nf) {
      F.lo[nf] = __builtin_bit_cast(s16x4, __builtin_amdgcn_ds_read_tr16_b64_v4i16(
          (__attribute__((address_space(3))) v4i16_t*)(wb + boff[nf][0])));
      F.hi[nf] = __builtin_bit_cast(s16x4, __builtin_amdgcn_ds_read_tr16_b64_v4i16(
          (__attribute__((address_space(3))) v4i16_t*)(wb + boff[nf][1])));
    }
  };
  auto mm = [&](const Frags& F) {
#pragma unroll
    for (int nf = 0; nf < 5; ++nf) {
      bf16x8 bfr = {F.lo[nf][0], F.lo[nf][1], F.lo[nf][2], F.lo[nf][3], F.hi[nf][0], F.hi[nf][1], F.hi[nf][2], F.hi[nf][3]};
#pragma unroll
      for (int m = 0; m < 4; ++m)
        acc[m][nf] = __builtin_amdgcn_mfma_f32_16x16x32_bf16(bfr, F.a[m], acc[m][nf], 0, 0, 0);
    }
  };
  Frags F[2];
  WAIT_L0;
  __builtin_amdgcn_s_barrier();
  rd(F[0], 0, 0);
#pragma unroll 1
  for (int gI = 0; gI < NIT / 4; ++gI) {
#pragma unroll
    for (int j = 0; j < 4; ++j) {
      WAIT_L0;
      __builtin_amdgcn_s_barrier();
      rd(F[(j + 1) & 1], (j + 1) & 3, (gI + ((j + 1) >> 2)) * 256);
      mm(F[j & 1]);
#pragma unroll
      for (int i = 0; i < 14; ++i) {
        __builtin_amdgcn_sched_group_barrier(0x100, 1, 0);
        __builtin_amdgcn_sched_group_barrier(0x8, 1, 0);
      }
      __builtin_amdgcn_sched_group_barrier(0x8, 6, 0);
      __builtin_amdgcn_sched_barrier(0);
    }
  }

  WAIT_L0;
  __builtin_amdgcn_s_barrier();
#pragma unroll
  for (int m = 0; m < 4; ++m)
#pragma unroll
    for (int n = 0; n < 5; ++n) {
      u32x2 o;
      o[0] = pack2((f32x2){acc[m][n][0], acc[m][n][1]});
      o[1] = pack2((f32x2){acc[m][n][2], acc[m][n][3]});
      *(u32x2*)(smem + (m * 16 + (lane & 15)) * 656 + (nq * 80 + n * 16 + fg * 4) * 2) = o;
    }
  }
  if (wid >= 4) __builtin_amdgcn_s_barrier();
  WAIT_L0;
  __builtin_amdgcn_s_barrier();
#pragma unroll
  for (int i = 0; i < 5; ++i) {
    const int id = tid + 512 * i;
    const int r = id / 40, c = id % 40;
    const u32x4 v = *(const u32x4*)(smem + r * 656 + c * 16);
    __builtin_nontemporal_store(v, (u32x4*)(out + (size_t)(row0 + r) * ND + col0 + c * 8));
  }
}

extern "C" void kernel_launch(void* const* d_in, const int* in_sizes, int n_in,
                              void* d_out, int out_size, void* d_ws, size_t ws_size,
                              hipStream_t stream) {
  const u16* x = (const u16*)d_in[0];
  const u16* g = (const u16*)d_in[1];
  const u16* b = (const u16*)d_in[2];
  const u16* w = (const u16*)d_in[3];
  u16* out = (u16*)d_out;
  const int M = in_sizes[0] / KD;
  ln_gemm_kernel<<<dim3((M / BM) * (ND / BN)), dim3(512), 0, stream>>>(x, g, b, w, out
  );
}
```

```cpp
#include <hip/hip_runtime.h>
#include <hip/hip_bf16.h>
#include <stdint.h>

typedef __attribute__((ext_vector_type(8))) short bf16x8;
typedef __attribute__((ext_vector_type(4))) short s16x4;
typedef __attribute__((ext_vector_type(4))) float f32x4;
typedef __attribute__((ext_vector_type(2))) float f32x2;
typedef __attribute__((ext_vector_type(4))) unsigned u32x4;
typedef __attribute__((ext_vector_type(2))) unsigned u32x2;
typedef __attribute__((ext_vector_type(2))) __bf16 bf16x2_t;
typedef __attribute__((ext_vector_type(4))) short v4i16_t;
typedef unsigned short u16;

constexpr int KD = 640, ND = 640;
constexpr int BM = 64, BN = 320, BK = 32, NST = 4, NIT = KD / BK;
constexpr int A_BYTES = BM * KD * 2;
constexpr int W_BYTES = BK * BN * 2;

__device__ __forceinline__ float dpp_sum16(float v) {
  int x;
  x = __builtin_amdgcn_update_dpp(0, __builtin_bit_cast(int, v), 0xB1, 0xF, 0xF, true);
  v += __builtin_bit_cast(float, x);
  x = __builtin_amdgcn_update_dpp(0, __builtin_bit_cast(int, v), 0x4E, 0xF, 0xF, true);
  v += __builtin_bit_cast(float, x);
  x = __builtin_amdgcn_update_dpp(0, __builtin_bit_cast(int, v), 0x141, 0xF, 0xF, true);
  v += __builtin_bit_cast(float, x);
  x = __builtin_amdgcn_update_dpp(0, __builtin_bit_cast(int, v), 0x140, 0xF, 0xF, true);
  v += __builtin_bit_cast(float, x);
  return v;
}
__device__ __forceinline__ f32x2 unpack2(unsigned u) {
  f32x2 r;
  r[0] = __builtin_bit_cast(float, u << 16);
  r[1] = __builtin_bit_cast(float, u & 0xffff0000u);
  return r;
}
__device__ __forceinline__ unsigned pack2(f32x2 f) {
  bf16x2_t r = __builtin_convertvector(f, bf16x2_t);
  return __builtin_bit_cast(unsigned, r);
}
__device__ __forceinline__ void glds16(const void* gsrc, unsigned lds_dst) {
  unsigned keep;
  asm volatile("s_mov_b32 %0, m0\n\ts_mov_b32 m0, %2\n\ts_nop 0\n\tglobal_load_lds_dwordx4 %1, off\n\ts_mov_b32 m0, %0"
               : "=&s"(keep) : "v"(gsrc), "s"(lds_dst) : "memory");
}
#define WAIT_V(n) asm volatile("s_waitcnt vmcnt(" #n ")" ::: "memory")
#define WAIT_L0 asm volatile("s_waitcnt lgkmcnt(0)" ::: "memory")

__global__ __launch_bounds__(512, 2) void ln_gemm_kernel(const u16* __restrict__ x, const u16* __restrict__ g,
                                                         const u16* __restrict__ b, const u16* __restrict__ w,
                                                         u16* __restrict__ out
) {
  __shared__ __attribute__((aligned(16))) char smem[A_BYTES + NST * W_BYTES];
  char* sA = smem;
  char* sW = smem + A_BYTES;
  const int tid = threadIdx.x, lane = tid & 63;
  const int wid = __builtin_amdgcn_readfirstlane(tid >> 6);
  const int bid = blockIdx.x, xcd = bid & 7, idx = bid >> 3;
  const int cb = idx & 1, rb = (idx >> 1) * 8 + xcd;
  const int row0 = rb * BM, col0 = cb * BN;

  const int rp = lane >> 3, pp = lane & 7;
  const int psw = pp ^ (((rp >> 1) & 3) << 1);
  const int pw = wid & 3;
  const u16* wsrc[5];
#pragma unroll
  for (int i = 0; i < 5; ++i) {
    const int blk = pw + 4 * i;
    const int kb = blk / 5, n64 = blk % 5;
    const int r = rp ^ ((kb & 1) << 2);
    wsrc[i] = w + (size_t)(kb * 8 + r) * ND + col0 + n64 * 64 + psw * 8;
  }
  const unsigned sWaddr = __builtin_amdgcn_readfirstlane((unsigned)(size_t)sW) + pw * 1024;
  auto dma = [&](int s) {
    const unsigned dst = sWaddr + (s & (NST - 1)) * W_BYTES;
    const size_t go = (size_t)s * BK * ND;
#pragma unroll
    for (int i = 0; i < 5; ++i) glds16(wsrc[i] + go, dst + i * 4096);
  };

  u32x4 wu[7];
#pragma unroll
  for (int i = 0; i < 7; ++i) wu[i] = (u32x4){0, 0, 0, 0};
  if (wid >= 4) {
    const char* wp = (const char*)w + (size_t)idx * 25600 + pw * 6400 + lane * 16;
#pragma unroll
    for (int i = 0; i < 7; ++i) {
      const int o = (i < 6) ? i * 1024 : 6400 - 1024;
      wu[i] = *(const u32x4*)(wp + o);
    }
  }
  const int cl = lane & 15, rq = lane >> 4;
  u32x4 xr[2][5], gr[5], br[5];
#pragma unroll
  for (int h = 0; h < 2; ++h)
#pragma unroll
    for (int i = 0; i < 5; ++i)
      xr[h][i] = *(const u32x4*)(x + (size_t)(row0 + wid * 8 + h * 4 + rq) * KD + (cl + 16 * i) * 8);
#pragma unroll
  for (int i = 0; i < 5; ++i) {
    gr[i] = *(const u32x4*)(g + (cl + 16 * i) * 8);
    br[i] = *(const u32x4*)(b + (cl + 16 * i) * 8);
  }
  if (wid >= 4) { dma(0); dma(1); dma(2); }

#pragma unroll
  for (int h = 0; h < 2; ++h) {
    f32x2 xv[5][4];
    f32x2 s2 = {0.f, 0.f};
#pragma unroll
    for (int i = 0; i < 5; ++i)
#pragma unroll
      for (int e = 0; e < 4; ++e) { const unsigned u = xr[h][i][e]; xv[i][e] = unpack2(u); s2 += xv[i][e]; }
    const float mean = dpp_sum16(s2[0] + s2[1]) * (1.0f / KD);
    const f32x2 m2 = {mean, mean};
    f32x2 q2 = {0.f, 0.f};
#pragma unroll
    for (int i = 0; i < 5; ++i)
#pragma unroll
      for (int e = 0; e < 4; ++e) { xv[i][e] -= m2; q2 += xv[i][e] * xv[i][e]; }
    const float rstd = rsqrtf(dpp_sum16(q2[0] + q2[1]) * (1.0f / KD) + 1e-5f);
    const f32x2 r2 = {rstd, rstd};
    const int r = wid * 8 + h * 4 + rq;
#pragma unroll
    for (int i = 0; i < 5; ++i) {
      u32x4 o;
#pragma unroll
      for (int e = 0; e < 4; ++e) {
        const unsigned gu = gr[i][e], bu = br[i][e];
        f32x2 y = (xv[i][e] * r2) * unpack2(gu) + unpack2(bu);
        o[e] = pack2(y);
      }
      *(u32x4*)(sA + r * 1280 + ((cl ^ (r & 15)) + 16 * i) * 16) = o;
    }
  }

  asm volatile("" :: "v"(wu[0]), "v"(wu[1]), "v"(wu[2]), "v"(wu[3]), "v"(wu[4]), "v"(wu[5]), "v"(wu[6]));
  if (wid >= 4) {
    WAIT_V(10);
    WAIT_L0;
    __builtin_amdgcn_s_barrier();
    dma(3);
#pragma unroll 1
    for (int s = 0; s < NIT; ++s) {
      if (s + 3 < NIT) WAIT_V(10);
      else if (s + 2 < NIT) WAIT_V(5);
      else WAIT_V(0);
      __builtin_amdgcn_s_barrier();
      if (s + 4 < NIT) dma(s + 4);
    }
  } else {
  const int nq = wid;
  const int fg = lane >> 4, fq = (lane & 15) >> 2, fp = lane & 3;
  f32x4 acc[4][5];
#pragma unroll
  for (int m = 0; m < 4; ++m)
#pragma unroll
    for (int n = 0; n < 5; ++n) acc[m][n] = (f32x4){0.f, 0.f, 0.f, 0.f};
  const int ar = (lane & 15);
  int boff[5][2];
#pragma unroll
  for (int nf = 0; nf < 5; ++nf) {
    const int c16 = nq * 5 + nf, n64 = c16 >> 2, j = c16 & 3;
#pragma unroll
    for (int t = 0; t < 2; ++t) {
      const int rpp = (4 * t + fq) ^ ((fg & 1) << 2);
      const int piece = (2 * j + (fp >> 1)) ^ (((rpp >> 1) & 3) << 1);
      boff[nf][t] = A_BYTES + (fg * 5 + n64) * 1024 + rpp * 128 + piece * 16 + (fp & 1) * 8;
    }
  }
  struct Frags { bf16x8 a[4]; s16x4 lo[5], hi[5]; };
  int aoff[4];
#pragma unroll
  for (int j = 0; j < 4; ++j) aoff[j] = ar * 1280 + (((4 * j + fg) ^ (ar & 15)) * 16);
  auto rd = [&](Frags& F, int j, int goff) {
    const char* wb = smem + j * W_BYTES;
    const char* ab = sA + aoff[j] + goff;
#pragma unroll
    for (int m = 0; m < 4; ++m) F.a[m] = *(const bf16x8*)(ab + m * 16 * 1280);
#pragma unroll
    for (int nf = 0; nf < 5; ++nf) {
      F.lo[nf] = __builtin_bit_cast(s16x4, __builtin_amdgcn_ds_read_tr16_b64_v4i16(
          (__attribute__((address_space(3))) v4i16_t*)(wb + boff[nf][0])));
      F.hi[nf] = __builtin_bit_cast(s16x4, __builtin_amdgcn_ds_read_tr16_b64_v4i16(
          (__attribute__((address_space(3))) v4i16_t*)(wb + boff[nf][1])));
    }
  };
  auto mm = [&](const Frags& F) {
#pragma unroll
    for (int nf = 0; nf < 5; ++nf) {
      bf16x8 bfr = {F.lo[nf][0], F.lo[nf][1], F.lo[nf][2], F.lo[nf][3], F.hi[nf][0], F.hi[nf][1], F.hi[nf][2], F.hi[nf][3]};
#pragma unroll
      for (int m = 0; m < 4; ++m)
        acc[m][nf] = __builtin_amdgcn_mfma_f32_16x16x32_bf16(bfr, F.a[m], acc[m][nf], 0, 0, 0);
    }
  };
  Frags F[2];
  WAIT_L0;
  __builtin_amdgcn_s_barrier();
  rd(F[0], 0, 0);
#pragma unroll 1
  for (int gI = 0; gI < NIT / 4; ++gI) {
#pragma unroll
    for (int j = 0; j < 4; ++j) {
      WAIT_L0;
      __builtin_amdgcn_s_barrier();
      rd(F[(j + 1) & 1], (j + 1) & 3, (gI + ((j + 1) >> 2)) * 256);
      mm(F[j & 1]);
#pragma unroll
      for (int i = 0; i < 14; ++i) {
        __builtin_amdgcn_sched_group_barrier(0x100, 1, 0);
        __builtin_amdgcn_sched_group_barrier(0x8, 1, 0);
      }
      __builtin_amdgcn_sched_group_barrier(0x8, 6, 0);
      __builtin_amdgcn_sched_barrier(0);
    }
  }

  WAIT_L0;
  __builtin_amdgcn_s_barrier();
#pragma unroll
  for (int m = 0; m < 4; ++m)
#pragma unroll
    for (int n = 0; n < 5; ++n) {
      u32x2 o;
      o[0] = pack2((f32x2){acc[m][n][0], acc[m][n][1]});
      o[1] = pack2((f32x2){acc[m][n][2], acc[m][n][3]});
      *(u32x2*)(smem + (m * 16 + (lane & 15)) * 656 + (nq * 80 + n * 16 + fg * 4) * 2) = o;
    }
  }
  if (wid >= 4) __builtin_amdgcn_s_barrier();
  WAIT_L0;
  __builtin_amdgcn_s_barrier();
#pragma unroll
  for (int i = 0; i < 5; ++i) {
    const int id = tid + 512 * i;
    const int r = id / 40, c = id % 40;
    const u32x4 v = *(const u32x4*)(smem + r * 656 + c * 16);
    __builtin_nontemporal_store(v, (u32x4*)(out + (size_t)(row0 + r) * ND + col0 + c * 8));
  }
}

extern "C" void kernel_launch(void* const* d_in, const int* in_sizes, int n_in,
                              void* d_out, int out_size, void* d_ws, size_t ws_size,
                              hipStream_t stream) {
  const u16* x = (const u16*)d_in[0];
  const u16* g = (const u16*)d_in[1];
  const u16* b = (const u16*)d_in[2];
  const u16* w = (const u16*)d_in[3];
  u16* out = (u16*)d_out;
  const int M = in_sizes[0] / KD;
  ln_gemm_kernel<<<dim3((M / BM) * (ND / BN)), dim3(512), 0, stream>>>(x, g, b, w, out
  );
}
```

```cpp
#include <hip/hip_runtime.h>
#include <hip/hip_bf16.h>
#include <stdint.h>

typedef __attribute__((ext_vector_type(8))) short bf16x8;
typedef __attribute__((ext_vector_type(4))) short s16x4;
typedef __attribute__((ext_vector_type(4))) float f32x4;
typedef __attribute__((ext_vector_type(2))) float f32x2;
typedef __attribute__((ext_vector_type(4))) unsigned u32x4;
typedef __attribute__((ext_vector_type(2))) unsigned u32x2;
typedef __attribute__((ext_vector_type(2))) __bf16 bf16x2_t;
typedef __attribute__((ext_vector_type(4))) short v4i16_t;
typedef unsigned short u16;

constexpr int KD = 640, ND = 640;
constexpr int BM = 64, BN = 320, BK = 32, NST = 4, NIT = KD / BK;
constexpr int A_BYTES = BM * KD * 2;
constexpr int W_BYTES = BK * BN * 2;

__device__ __forceinline__ float dpp_sum16(float v) {
  int x;
  x = __builtin_amdgcn_update_dpp(0, __builtin_bit_cast(int, v), 0xB1, 0xF, 0xF, true);
  v += __builtin_bit_cast(float, x);
  x = __builtin_amdgcn_update_dpp(0, __builtin_bit_cast(int, v), 0x4E, 0xF, 0xF, true);
  v += __builtin_bit_cast(float, x);
  x = __builtin_amdgcn_update_dpp(0, __builtin_bit_cast(int, v), 0x141, 0xF, 0xF, true);
  v += __builtin_bit_cast(float, x);
  x = __builtin_amdgcn_update_dpp(0, __builtin_bit_cast(int, v), 0x140, 0xF, 0xF, true);
  v += __builtin_bit_cast(float, x);
  return v;
}
__device__ __forceinline__ f32x2 unpack2(unsigned u) {
  f32x2 r;
  r[0] = __builtin_bit_cast(float, u << 16);
  r[1] = __builtin_bit_cast(float, u & 0xffff0000u);
  return r;
}
__device__ __forceinline__ unsigned pack2(f32x2 f) {
  bf16x2_t r = __builtin_convertvector(f, bf16x2_t);
  return __builtin_bit_cast(unsigned, r);
}
__device__ __forceinline__ void glds16(const void* gsrc, unsigned lds_dst) {
  unsigned keep;
  asm volatile("s_mov_b32 %0, m0\n\ts_mov_b32 m0, %2\n\ts_nop 0\n\tglobal_load_lds_dwordx4 %1, off\n\ts_mov_b32 m0, %0"
               : "=&s"(keep) : "v"(gsrc), "s"(lds_dst) : "memory");
}
#define WAIT_V(n) asm volatile("s_waitcnt vmcnt(" #n ")" ::: "memory")
#define WAIT_L0 asm volatile("s_waitcnt lgkmcnt(0)" ::: "memory")

__global__ __launch_bounds__(512, 2) void ln_gemm_kernel(const u16* __restrict__ x, const u16* __restrict__ g,
                                                         const u16* __restrict__ b, const u16* __restrict__ w,
                                                         u16* __restrict__ out) {
  __shared__ __attribute__((aligned(16))) char smem[A_BYTES + NST * W_BYTES];
  char* sA = smem;
  char* sW = smem + A_BYTES;
  const int tid = threadIdx.x, lane = tid & 63;
  const int wid = __builtin_amdgcn_readfirstlane(tid >> 6);
  const int bid = blockIdx.x, xcd = bid & 7, idx = bid >> 3;
  const int cb = idx & 1, rb = (idx >> 1) * 8 + xcd;
  const int row0 = rb * BM, col0 = cb * BN;

  const int rp = lane >> 3, pp = lane & 7;
  const int psw = pp ^ (((rp >> 1) & 3) << 1);
  const int pw = wid & 3;
  const u16* wsrc[5];
#pragma unroll
  for (int i = 0; i < 5; ++i) {
    const int blk = pw + 4 * i;
    const int kb = blk / 5, n64 = blk % 5;
    const int r = rp ^ ((kb & 1) << 2);
    wsrc[i] = w + (size_t)(kb * 8 + r) * ND + col0 + n64 * 64 + psw * 8;
  }
  const unsigned sWaddr = __builtin_amdgcn_readfirstlane((unsigned)(size_t)sW) + pw * 1024;
  auto dma = [&](int s) {
    const unsigned dst = sWaddr + (s & (NST - 1)) * W_BYTES;
    const size_t go = (size_t)s * BK * ND;
#pragma unroll
    for (int i = 0; i < 5; ++i) glds16(wsrc[i] + go, dst + i * 4096);
  };

  u32x4 wu[7];
#pragma unroll
  for (int i = 0; i < 7; ++i) wu[i] = (u32x4){0, 0, 0, 0};
  if (wid >= 4) {
    const char* wp = (const char*)w + (size_t)idx * 25600 + pw * 6400 + lane * 16;
#pragma unroll
    for (int i = 0; i < 7; ++i) {
      const int o = (i < 6) ? i * 1024 : 6400 - 1024;
      wu[i] = *(const u32x4*)(wp + o);
    }
  }
  const int cl = lane & 15, rq = lane >> 4;
  u32x4 xr[2][5], gr[5], br[5];
#pragma unroll
  for (int h = 0; h < 2; ++h)
#pragma unroll
    for (int i = 0; i < 5; ++i)
      xr[h][i] = *(const u32x4*)(x + (size_t)(row0 + wid * 8 + h * 4 + rq) * KD + (cl + 16 * i) * 8);
#pragma unroll
  for (int i = 0; i < 5; ++i) {
    gr[i] = *(const u32x4*)(g + (cl + 16 * i) * 8);
    br[i] = *(const u32x4*)(b + (cl + 16 * i) * 8);
  }
  if (wid >= 4) { dma(0); dma(1); dma(2); }

#pragma unroll
  for (int h = 0; h < 2; ++h) {
    f32x2 xv[5][4];
    f32x2 s2 = {0.f, 0.f};
#pragma unroll
    for (int i = 0; i < 5; ++i)
#pragma unroll
      for (int e = 0; e < 4; ++e) { const unsigned u = xr[h][i][e]; xv[i][e] = unpack2(u); s2 += xv[i][e]; }
    const float mean = dpp_sum16(s2[0] + s2[1]) * (1.0f / KD);
    const f32x2 m2 = {mean, mean};
    f32x2 q2 = {0.f, 0.f};
#pragma unroll
    for (int i = 0; i < 5; ++i)
#pragma unroll
      for (int e = 0; e < 4; ++e) { xv[i][e] -= m2; q2 += xv[i][e] * xv[i][e]; }
    const float rstd = rsqrtf(dpp_sum16(q2[0] + q2[1]) * (1.0f / KD) + 1e-5f);
    const f32x2 r2 = {rstd, rstd};
    const int r = wid * 8 + h * 4 + rq;
#pragma unroll
    for (int i = 0; i < 5; ++i) {
      u32x4 o;
#pragma unroll
      for (int e = 0; e < 4; ++e) {
        const unsigned gu = gr[i][e], bu = br[i][e];
        f32x2 y = (xv[i][e] * r2) * unpack2(gu) + unpack2(bu);
        o[e] = pack2(y);
      }
      *(u32x4*)(sA + r * 1280 + ((cl ^ (r & 15)) + 16 * i) * 16) = o;
    }
  }

  asm volatile("" :: "v"(wu[0]), "v"(wu[1]), "v"(wu[2]), "v"(wu[3]), "v"(wu[4]), "v"(wu[5]), "v"(wu[6]));
  if (wid >= 4) {
    WAIT_V(10);
    WAIT_L0;
    __builtin_amdgcn_s_barrier();
    dma(3);
#pragma unroll 1
    for (int s = 0; s < NIT; ++s) {
      if (s + 3 < NIT) WAIT_V(10);
      else if (s + 2 < NIT) WAIT_V(5);
      else WAIT_V(0);
      __builtin_amdgcn_s_barrier();
      if (s + 4 < NIT) dma(s + 4);
    }
  } else {
  const int nq = wid;
  const int fg = lane >> 4, fq = (lane & 15) >> 2, fp = lane & 3;
  f32x4 acc[4][5];
#pragma unroll
  for (int m = 0; m < 4; ++m)
#pragma unroll
    for (int n = 0; n < 5; ++n) acc[m][n] = (f32x4){0.f, 0.f, 0.f, 0.f};
  const int ar = (lane & 15);
  int boff[5][2];
#pragma unroll
  for (int nf = 0; nf < 5; ++nf) {
    const int c16 = nq * 5 + nf, n64 = c16 >> 2, j = c16 & 3;
#pragma unroll
    for (int t = 0; t < 2; ++t) {
      const int rpp = (4 * t + fq) ^ ((fg & 1) << 2);
      const int piece = (2 * j + (fp >> 1)) ^ (((rpp >> 1) & 3) << 1);
      boff[nf][t] = A_BYTES + (fg * 5 + n64) * 1024 + rpp * 128 + piece * 16 + (fp & 1) * 8;
    }
  }
  struct Frags { bf16x8 a[4]; s16x4 lo[5], hi[5]; };
  int aoff[4];
#pragma unroll
  for (int j = 0; j < 4; ++j) aoff[j] = ar * 1280 + (((4 * j + fg) ^ (ar & 15)) * 16);
  auto rd = [&](Frags& F, int j, int goff) {
    const char* wb = smem + j * W_BYTES;
    const char* ab = sA + aoff[j] + goff;
#pragma unroll
    for (int m = 0; m < 4; ++m) F.a[m] = *(const bf16x8*)(ab + m * 16 * 1280);
#pragma unroll
    for (int nf = 0; nf < 5; ++nf) {
      F.lo[nf] = __builtin_bit_cast(s16x4, __builtin_amdgcn_ds_read_tr16_b64_v4i16(
          (__attribute__((address_space(3))) v4i16_t*)(wb + boff[nf][0])));
      F.hi[nf] = __builtin_bit_cast(s16x4, __builtin_amdgcn_ds_read_tr16_b64_v4i16(
          (__attribute__((address_space(3))) v4i16_t*)(wb + boff[nf][1])));
    }
  };
  auto mm = [&](const Frags& F) {
#pragma unroll
    for (int nf = 0; nf < 5; ++nf) {
      bf16x8 bfr = {F.lo[nf][0], F.lo[nf][1], F.lo[nf][2], F.lo[nf][3], F.hi[nf][0], F.hi[nf][1], F.hi[nf][2], F.hi[nf][3]};
#pragma unroll
      for (int m = 0; m < 4; ++m)
        acc[m][nf] = __builtin_amdgcn_mfma_f32_16x16x32_bf16(bfr, F.a[m], acc[m][nf], 0, 0, 0);
    }
  };
  Frags F[2];
  WAIT_L0;
  __builtin_amdgcn_s_barrier();
  rd(F[0], 0, 0);
#pragma unroll 1
  for (int gI = 0; gI < NIT / 4; ++gI) {
#pragma unroll
    for (int j = 0; j < 4; ++j) {
      WAIT_L0;
      __builtin_amdgcn_s_barrier();
      rd(F[(j + 1) & 1], (j + 1) & 3, (gI + ((j + 1) >> 2)) * 256);
      mm(F[j & 1]);
#pragma unroll
      for (int i = 0; i < 14; ++i) {
        __builtin_amdgcn_sched_group_barrier(0x100, 1, 0);
        __builtin_amdgcn_sched_group_barrier(0x8, 1, 0);
      }
      __builtin_amdgcn_sched_group_barrier(0x8, 6, 0);
      __builtin_amdgcn_sched_barrier(0);
    }
  }

  WAIT_L0;
  __builtin_amdgcn_s_barrier();
#pragma unroll
  for (int m = 0; m < 4; ++m)
#pragma unroll
    for (int n = 0; n < 5; ++n) {
      u32x2 o;
      o[0] = pack2((f32x2){acc[m][n][0], acc[m][n][1]});
      o[1] = pack2((f32x2){acc[m][n][2], acc[m][n][3]});
      *(u32x2*)(smem + (m * 16 + (lane & 15)) * 656 + (nq * 80 + n * 16 + fg * 4) * 2) = o;
    }
  }
  if (wid >= 4) __builtin_amdgcn_s_barrier();
  WAIT_L0;
  __builtin_amdgcn_s_barrier();
#pragma unroll
  for (int i = 0; i < 5; ++i) {
    const int id = tid + 512 * i;
    const int r = id / 40, c = id % 40;
    const u32x4 v = *(const u32x4*)(smem + r * 656 + c * 16);
    __builtin_nontemporal_store(v, (u32x4*)(out + (size_t)(row0 + r) * ND + col0 + c * 8));
  }
}

extern "C" void kernel_launch(void* const* d_in, const int* in_sizes, int n_in,
                              void* d_out, int out_size, void* d_ws, size_t ws_size,
                              hipStream_t stream) {
  const u16* x = (const u16*)d_in[0];
  const u16* g = (const u16*)d_in[1];
  const u16* b = (const u16*)d_in[2];
  const u16* w = (const u16*)d_in[3];
  u16* out = (u16*)d_out;
  const int M = in_sizes[0] / KD;
  ln_gemm_kernel<<<dim3((M / BM) * (ND / BN)), dim3(512), 0, stream>>>(x, g, b, w, out);
}
```
